```python
import torch

def _softmax(x, axis=-1):
    return torch.softmax(x, dim=axis)
def _gelu(x):
    return torch.nn.functional.gelu(x, approximate="tanh")
def _silu(x):
    return torch.nn.functional.silu(x)
def _sigmoid(x):
    return torch.sigmoid(x)
def _layernorm(x, axis=-1, eps=1e-5):
    m = x.mean(dim=axis, keepdim=True)
    v = x.var(dim=axis, keepdim=True, unbiased=False)
    return (x - m) / torch.sqrt(v + eps)
def _scatter_add(tgt, idx, src, axis=0):
    return tgt.clone().index_add_(axis, idx, src)
def _segment_sum(src, seg, n):
    out = torch.zeros((n,) + src.shape[1:], dtype=src.dtype)
    return out.index_add_(0, seg, src)
def _pad(x, pw):
    flat = tuple(v for pair in reversed(pw) for v in pair)
    return torch.nn.functional.pad(x, flat)

kernel_name = 'parallel_sum__mha_block_cached__geglu_ffn__decode'

def setup_inputs():

    _w = (lambda a: (lambda t: t.to(torch.bfloat16) if t.is_floating_point() else t)(torch.as_tensor(a)))
    def _A_setup():
        B, Sq, D = 4, 1, 448
        H, Sk, Dh = 16, 4096, 256
        return {
            "x": _w((lambda *s: torch.randn(*s, dtype=torch.bfloat16) * 0.05)(B, Sq, D)),
            "k_cache": _w((lambda *s: torch.randn(*s, dtype=torch.bfloat16) * 3.2)(B, H, Sk, Dh)),
            "v_cache": _w((lambda *s: torch.randn(*s, dtype=torch.bfloat16) * 1.6)(B, H, Sk, Dh)),
            "wq": _w((lambda *s: torch.randn(*s, dtype=torch.bfloat16) * 3.2)(D, H * Dh) * (D ** -0.5)),
            "wo": _w((lambda *s: torch.randn(*s, dtype=torch.bfloat16) * 1.6)(H * Dh, D) * ((H * Dh) ** -0.5)),
        }
    _A_in = _A_setup()

    def _B_setup():
        B, S, D, F = 4, 1, 448, 1792
        return {
            "x": _w((lambda *s: torch.randn(*s, dtype=torch.bfloat16) * 0.05)(B, S, D)),
            "w_gate": _w((lambda *s: torch.randn(*s, dtype=torch.bfloat16) * 0.05)(D, F)),
            "w_up": _w((lambda *s: torch.randn(*s, dtype=torch.bfloat16) * 0.05)(D, F)),
            "w_down": _w((lambda *s: torch.randn(*s, dtype=torch.bfloat16) * 0.05)(F, D)),
        }
    _B_in = _B_setup()

    return {
        "A_x": _A_in["x"],
        "A_k_cache": _A_in["k_cache"],
        "A_v_cache": _A_in["v_cache"],
        "A_wq": _A_in["wq"],
        "A_wo": _A_in["wo"],
        "B_x": _B_in["x"],
        "B_w_gate": _B_in["w_gate"],
        "B_w_up": _B_in["w_up"],
        "B_w_down": _B_in["w_down"],
    }

def reference(A_x, A_k_cache, A_v_cache, A_wq, A_wo, B_x, B_w_gate, B_w_up, B_w_down):
    def _A_ref(x, k_cache, v_cache, wq, wo):
        B, Sq, D = x.shape
        H, Dh = 16, 256
        q = (lambda x, *d: x.permute(*d))((x @ wq).reshape(B, Sq, H, Dh), 0, 2, 1, 3)
        scores = torch.einsum("bhqd,bhkd->bhqk", q, k_cache) * (Dh ** -0.5)
        p = _softmax(scores, axis=-1)
        o = torch.einsum("bhqk,bhkd->bhqd", p, v_cache)
        return (lambda x, *d: x.permute(*d))(o, 0, 2, 1, 3).reshape(B, Sq, H * Dh) @ wo

    def _B_ref(x, w_gate, w_up, w_down):
        return (_gelu(x @ w_gate) * (x @ w_up)) @ w_down

    x = A_x
    xn = _layernorm(x, axis=-1)
    return x + _A_ref(xn, A_k_cache, A_v_cache, A_wq, A_wo) + _B_ref(xn, B_w_gate, B_w_up, B_w_down)
```

```cpp
#include <hip/hip_runtime.h>
#include <hip/hip_bf16.h>
#include <stdint.h>

#define MIMIC 0

using u32x4 = __attribute__((ext_vector_type(4))) unsigned;
using bf16x2 = __attribute__((ext_vector_type(2))) __bf16;
typedef unsigned short u16;

constexpr int B = 4, D = 448, H = 16, SK = 4096, DH = 256, F = 1792;
constexpr int NSPLIT = 4;

__device__ __forceinline__ float bf2f(u16 v) { return __uint_as_float(((unsigned)v) << 16); }
__device__ __forceinline__ float rbf(float f) { return (float)(__bf16)f; }
__device__ __forceinline__ u16 f2bf(float f) { __bf16 b = (__bf16)f; return __builtin_bit_cast(u16, b); }
__device__ __forceinline__ float dot2(unsigned k, unsigned q, float acc) {
  return __builtin_amdgcn_fdot2_f32_bf16(__builtin_bit_cast(bf16x2, k), __builtin_bit_cast(bf16x2, q), acc, false);
}

__device__ void layernorm_to_lds(const u16* x, float* xn  , float* red, int tid, int nthr) {
  int lane = tid & 63, wave = tid >> 6, nw = nthr >> 6;
  for (int b = wave; b < B; b += nw) {
    float v[7]; float s = 0.f;
    for (int i = 0; i < 7; ++i) { v[i] = bf2f(x[b * D + lane + 64 * i]); s += v[i]; }
    for (int o = 32; o; o >>= 1) s += __shfl_xor(s, o);
    float mean = s / D;
    float ss = 0.f;
    for (int i = 0; i < 7; ++i) { float d = v[i] - mean; ss += d * d; }
    for (int o = 32; o; o >>= 1) ss += __shfl_xor(ss, o);
    float var = ss / D;
    float den = sqrtf(var + 1e-5f);
    for (int i = 0; i < 7; ++i) xn[b * D + lane + 64 * i] = (v[i] - mean) / den;
  }
  __syncthreads();
}

__global__ void __launch_bounds__(256) k_pro(const u16* x, const u16* wq, const u16* wg, const u16* wu, float* q, float* hbuf) {
  __shared__ float xn[B * D];
  __shared__ float red[4][2][B][64];
  int tid = threadIdx.x, lane = tid & 63, wave = tid >> 6;
  layernorm_to_lds(x, xn, nullptr, tid, 256);
  int blk = blockIdx.x;
  if (blk < 64) {
    int n = blk * 64 + lane;
    float a[B] = {0, 0, 0, 0};
    for (int k = wave * 112; k < wave * 112 + 112; ++k) {
      float w = bf2f(wq[(size_t)k * 4096 + n]);
      for (int b = 0; b < B; ++b) a[b] = fmaf(xn[b * D + k], w, a[b]);
    }
    for (int b = 0; b < B; ++b) red[wave][0][b][lane] = a[b];
    __syncthreads();
    int b = wave;
    float s = red[0][0][b][lane] + red[1][0][b][lane] + red[2][0][b][lane] + red[3][0][b][lane];
    q[b * 4096 + n] = rbf(s);
  } else {
    int n = (blk - 64) * 64 + lane;
    float a[B] = {0, 0, 0, 0}, c[B] = {0, 0, 0, 0};
    for (int k = wave * 112; k < wave * 112 + 112; ++k) {
      float w = bf2f(wg[(size_t)k * F + n]);
      float u = bf2f(wu[(size_t)k * F + n]);
      for (int b = 0; b < B; ++b) { a[b] = fmaf(xn[b * D + k], w, a[b]); c[b] = fmaf(xn[b * D + k], u, c[b]); }
    }
    for (int b = 0; b < B; ++b) { red[wave][0][b][lane] = a[b]; red[wave][1][b][lane] = c[b]; }
    __syncthreads();
    int b = wave;
    float g = red[0][0][b][lane] + red[1][0][b][lane] + red[2][0][b][lane] + red[3][0][b][lane];
    float u = red[0][1][b][lane] + red[1][1][b][lane] + red[2][1][b][lane] + red[3][1][b][lane];
    float inner = 0.7978845608028654f * (g + 0.044715f * g * g * g);
    float ge = 0.5f * g * (1.f + tanhf(inner));
    hbuf[b * F + n] = ge * u;
  }
}

__global__ void __launch_bounds__(512) k_attn(const u16* kc, const u16* vc, const float* q, float* pm, float* pl, float* pacc) {
  __shared__ float sm[8], sl[8], sacc[8][DH];
  __shared__ float pbuf[8][32];
  int tid = threadIdx.x, lane = tid & 63, wave = tid >> 6;
  int blk = blockIdx.x; int s = blk & 3, bh = blk >> 2;
  int c = lane & 31, r = lane >> 5;
  unsigned qp[4];
  for (int i = 0; i < 4; ++i) {
    float lo = q[bh * 256 + 8 * c + 2 * i], hi = q[bh * 256 + 8 * c + 2 * i + 1];
    qp[i] = (unsigned)f2bf(lo) | ((unsigned)f2bf(hi) << 16);
  }
  const u16* kb = kc + ((size_t)bh * SK + s * 1024 + wave * 128) * DH;
  const u16* vb = vc + ((size_t)bh * SK + s * 1024 + wave * 128) * DH;
  float m = -INFINITY, l = 0.f; float acc[8] = {0, 0, 0, 0, 0, 0, 0, 0};
  for (int t = 0; t < 64; ++t) {
    u32x4 kk = *(const u32x4*)(kb + (size_t)(2 * t + r) * DH + 8 * c);
    u32x4 vv = *(const u32x4*)(vb + (size_t)(2 * t + r) * DH + 8 * c);
    float d = 0.f;
    for (int i = 0; i < 4; ++i) d = dot2(kk[i], qp[i], d);
    for (int o = 16; o; o >>= 1) d += __shfl_xor(d, o);
    d *= 0.0625f;
    float mn = fmaxf(m, d);
    float al = __expf(m - mn), p = __expf(d - mn);
    l = l * al + p; m = mn;
    for (int i = 0; i < 4; ++i) {
      acc[2 * i] = acc[2 * i] * al + p * __uint_as_float(vv[i] << 16);
      acc[2 * i + 1] = acc[2 * i + 1] * al + p * __uint_as_float(vv[i] & 0xffff0000u);
    }
  }
  float mo = __shfl_xor(m, 32), lo = __shfl_xor(l, 32);
  float mn = fmaxf(m, mo); float a0 = __expf(m - mn), a1 = __expf(mo - mn);
  l = l * a0 + lo * a1;
  for (int i = 0; i < 8; ++i) { float o = __shfl_xor(acc[i], 32); acc[i] = acc[i] * a0 + o * a1; }
  m = mn;
  if (r == 0) for (int i = 0; i < 8; ++i) sacc[wave][8 * c + i] = acc[i];
  if (lane == 0) { sm[wave] = m; sl[wave] = l; }
  __syncthreads();
  if (tid < DH) {
    float M = sm[0]; for (int w = 1; w < 8; ++w) M = fmaxf(M, sm[w]);
    float L = 0.f, o = 0.f;
    for (int w = 0; w < 8; ++w) { float e = __expf(sm[w] - M); L += e * sl[w]; o += e * sacc[w][tid]; }
    pacc[(size_t)blk * DH + tid] = o;
    if (tid == 0) { pm[blk] = M; pl[blk] = L; }
  }
}

__global__ void __launch_bounds__(512) k_fin(const u16* x, const float* pm, const float* pl, const float* pacc, const u16* wo,
                                             const float* hbuf, const u16* wd, u16* out) {
  __shared__ float o[4096];
  __shared__ float hh[F];
  __shared__ float red[2][8][64];
  int tid = threadIdx.x, lane = tid & 63, wave = tid >> 6;
  int b = blockIdx.x / 7, cb = blockIdx.x % 7;
  for (int i = tid; i < 4096; i += 512) {
    int h = i >> 8, d = i & 255; int p0 = (b * 16 + h) * 4;
    float M = fmaxf(fmaxf(pm[p0], pm[p0 + 1]), fmaxf(pm[p0 + 2], pm[p0 + 3]));
    float L = 0.f, a = 0.f;
    for (int s = 0; s < 4; ++s) { float e = __expf(pm[p0 + s] - M); L += e * pl[p0 + s]; a += e * pacc[(size_t)(p0 + s) * DH + d]; }
    a = a / L;
    o[i] = a;
  }
  for (int i = tid; i < F; i += 512) hh[i] = hbuf[b * F + i];
  __syncthreads();
  int n = cb * 64 + lane;
  float a = 0.f, f = 0.f;
  for (int k = wave * 512; k < wave * 512 + 512; ++k) a = fmaf(o[k], bf2f(wo[(size_t)k * D + n]), a);
  for (int k = wave * 224; k < wave * 224 + 224; ++k) f = fmaf(hh[k], bf2f(wd[(size_t)k * D + n]), f);
  red[0][wave][lane] = a; red[1][wave][lane] = f;
  __syncthreads();
  if (tid < 64) {
    float A = 0.f, Fv = 0.f;
    for (int w = 0; w < 8; ++w) { A += red[0][w][lane]; Fv += red[1][w][lane]; }
    float xv = bf2f(x[b * D + n]);
    float r = xv + A + Fv;
    out[b * D + n] = f2bf(r);
  }
}

extern "C" void kernel_launch(void* const* d_in, const int* in_sizes, int n_in, void* d_out, int out_size, void* d_ws, size_t ws_size,
                              hipStream_t stream) {
  const u16* x = (const u16*)d_in[0];
  const u16* kc = (const u16*)d_in[1];
  const u16* vc = (const u16*)d_in[2];
  const u16* wq = (const u16*)d_in[3];
  const u16* wo = (const u16*)d_in[4];
  const u16* wg = (const u16*)d_in[6];
  const u16* wu = (const u16*)d_in[7];
  const u16* wd = (const u16*)d_in[8];
  float* ws = (float*)d_ws;
  float* q = ws;
  float* hbuf = ws + 16384;
  float* pm = ws + 32768;
  float* pl = ws + 33024;
  float* pacc = ws + 36864;
  k_pro<<<64 + 28, 256, 0, stream>>>(x, wq, wg, wu, q, hbuf);
  k_attn<<<256, 512, 0, stream>>>(kc, vc, q, pm, pl, pacc);
  k_fin<<<28, 512, 0, stream>>>(x, pm, pl, pacc, wo, hbuf, wd, (u16*)d_out);
}
```

```cpp
#include <hip/hip_runtime.h>
#include <hip/hip_bf16.h>
#include <stdint.h>

constexpr int AUXK = 2, AUXV = 2;
constexpr int AUXWQ = 0, AUXWF = 0, AUXWO = 0;
constexpr int PFD = 6;
constexpr int PFI = 3;
constexpr int WST = 8;
constexpr int WOCH = 5;
constexpr int HIFG = 2;
constexpr int HIFG3 = 4;
constexpr int HIFW = 3;
constexpr int SPRIO = 3;

typedef unsigned u32;
typedef unsigned short u16;
using u32x4 = __attribute__((ext_vector_type(4))) unsigned;
using u32x2 = __attribute__((ext_vector_type(2))) unsigned;
using f32x4 = __attribute__((ext_vector_type(4))) float;
using f32x2 = __attribute__((ext_vector_type(2))) float;
using bf16x2 = __attribute__((ext_vector_type(2))) __bf16;
typedef __amdgpu_buffer_rsrc_t rsrc_t;

constexpr int D = 448, NH = 16, SK = 4096, DH = 256, F = 1792;
constexpr float CEXP = 1.4426950408889634f / 16.f;
constexpr u32 WS_E = 0, WS_ML = 4096, WS_Q = 65536, WS_PART = 131072, WS_BYTES = WS_PART + 256 * 448 * 16;

__device__ __forceinline__ float asf(u32 v) { return __uint_as_float(v); }
__device__ __forceinline__ u32 asu(float v) { return __float_as_uint(v); }
__device__ __forceinline__ float bflo(u32 v) { return asf(v << 16); }
__device__ __forceinline__ float bfhi(u32 v) { return asf(v & 0xffff0000u); }
__device__ __forceinline__ u32 pkbf(float lo, float hi) {
  f32x2 f = {lo, hi};
  return __builtin_bit_cast(u32, __builtin_convertvector(f, bf16x2));
}
__device__ __forceinline__ float rbf(float f) { return asf(pkbf(f, 0.f) << 16); }
__device__ __forceinline__ float dot2(u32 a, u32 b, float c) {
  return __builtin_amdgcn_fdot2_f32_bf16(__builtin_bit_cast(bf16x2, a), __builtin_bit_cast(bf16x2, b), c, false);
}
template <int CTRL> __device__ __forceinline__ float dpp(float x) { return asf(__builtin_amdgcn_mov_dpp(asu(x), CTRL, 0xf, 0xf, true)); }
constexpr int DPP_X1 = 0xB1, DPP_X2 = 0x4E, DPP_ROR4 = 0x124, DPP_ROR8 = 0x128, DPP_HMIR = 0x141, DPP_BC = 0x150;
__device__ __forceinline__ float sum16(float a, float b) {
  u32x2 r = __builtin_amdgcn_permlane16_swap(asu(a), asu(b), false, false); return asf(r[0]) + asf(r[1]); }
__device__ __forceinline__ float sum32(float a, float b) {
  u32x2 r = __builtin_amdgcn_permlane32_swap(asu(a), asu(b), false, false); return asf(r[0]) + asf(r[1]); }
__device__ __forceinline__ float max16(float a) {
  u32x2 r = __builtin_amdgcn_permlane16_swap(asu(a), asu(a), false, false); return fmaxf(asf(r[0]), asf(r[1])); }
__device__ __forceinline__ float max32(float a) {
  u32x2 r = __builtin_amdgcn_permlane32_swap(asu(a), asu(a), false, false); return fmaxf(asf(r[0]), asf(r[1])); }
__device__ __forceinline__ float wavesum(float v) {
  v += dpp<DPP_X1>(v); v += dpp<DPP_X2>(v); v += dpp<DPP_ROR4>(v); v += dpp<DPP_ROR8>(v);
  v = sum16(v, v); return sum32(v, v); }
__device__ __forceinline__ u32 prlo(u32 hiw, u32 low) { return __builtin_amdgcn_perm(hiw, low, 0x05040100u); }
__device__ __forceinline__ u32 prhi(u32 hiw, u32 low) { return __builtin_amdgcn_perm(hiw, low, 0x07060302u); }
__device__ __forceinline__ rsrc_t mkrsrc(const void* p, u32 bytes) { return __builtin_amdgcn_make_buffer_rsrc((void*)p, 0, bytes, 0x00020000); }
template <int AUX> __device__ __forceinline__ u32x4 ld16(rsrc_t r, u32 voff, u32 soff) { return __builtin_amdgcn_raw_buffer_load_b128(r, voff, soff, AUX); }
#define MEMFENCE() asm volatile("" ::: "memory")
#define SCHED() do { asm volatile("" ::: "memory"); __builtin_amdgcn_sched_barrier(0); } while (0)
template <int N> __device__ __forceinline__ void vmwait() { asm volatile("s_waitcnt vmcnt(%0)" :: "n"(N) : "memory"); __builtin_amdgcn_sched_barrier(0); }
#define INFLIGHT(i, n) do { if ((i) >= (n)) vmwait<((n) - 1 < 63 ? (n) - 1 : 63)>(); } while (0)
__device__ __forceinline__ void blk_barrier() { asm volatile("s_waitcnt lgkmcnt(0)\n\ts_barrier" ::: "memory"); }

struct Lds {
  u16 xnb[4][D];
  u16 xraw[D];
  float qpart[4][64];
  u32 qb[128];
  float gup[4][32];
  float zpart[4][D];
  float accp[4][DH];
  float ml[4][2];
  float ypart[8][D];
  u32 tag;
  u32 cnt;
  float fin[8][16];
};

template <bool REFILL>
__device__ __forceinline__ void tile(u32x4 (&K)[4], u32x4 (&V)[4], const u32 (&q)[4], float& m, float& l, float (&acc)[8],
                                     rsrc_t rk, rsrc_t rv, u32 voff, u32 snext, bool b4, bool b8) {
  float d[4];
#pragma unroll
  for (int i = 0; i < 4; ++i) d[i] = dot2(K[i][0], q[0], 0.f);
#pragma unroll
  for (int j = 1; j < 4; ++j)
#pragma unroll
    for (int i = 0; i < 4; ++i) d[i] = dot2(K[i][j], q[j], d[i]);
  if (REFILL) {
    asm volatile("" : "+v"(d[0]), "+v"(d[1]), "+v"(d[2]), "+v"(d[3]) :: "memory");
    SCHED();
#pragma unroll
    for (int i = 0; i < 4; ++i) K[i] = ld16<AUXK>(rk, voff + i * 1024, snext);
    SCHED();
  }
  float k01 = b4 ? d[1] : d[0], s01 = b4 ? d[0] : d[1];
  float k23 = b4 ? d[3] : d[2], s23 = b4 ? d[2] : d[3];
  float r01 = k01 + dpp<DPP_HMIR>(s01);
  float r23 = k23 + dpp<DPP_HMIR>(s23);
  float kk = b8 ? r23 : r01, ss = b8 ? r01 : r23;
  float r = kk + dpp<DPP_ROR8>(ss);
  r += dpp<DPP_X1>(r);
  r += dpp<DPP_X2>(r);
  r = sum16(r, r);
  const float s = rbf(r);
  float mx = fmaxf(s, dpp<DPP_ROR4>(s));
  mx = fmaxf(mx, dpp<DPP_ROR8>(mx));
  const float mn = fmaxf(m, mx);
  const float al = __builtin_amdgcn_exp2f((m - mn) * CEXP);
  const float p = __builtin_amdgcn_exp2f((s - mn) * CEXP);
  m = mn;
  l = fmaf(l, al, p);
  const float p0 = dpp<DPP_BC + 0>(p), p1 = dpp<DPP_BC + 4>(p), p2 = dpp<DPP_BC + 8>(p), p3 = dpp<DPP_BC + 12>(p);
#pragma unroll
  for (int j = 0; j < 4; ++j) {
    acc[2 * j] = fmaf(p0, bflo(V[0][j]), acc[2 * j] * al);
    acc[2 * j + 1] = fmaf(p0, bfhi(V[0][j]), acc[2 * j + 1] * al);
  }
#pragma unroll
  for (int j = 0; j < 4; ++j) { acc[2 * j] = fmaf(p1, bflo(V[1][j]), acc[2 * j]); acc[2 * j + 1] = fmaf(p1, bfhi(V[1][j]), acc[2 * j + 1]); }
#pragma unroll
  for (int j = 0; j < 4; ++j) { acc[2 * j] = fmaf(p2, bflo(V[2][j]), acc[2 * j]); acc[2 * j + 1] = fmaf(p2, bfhi(V[2][j]), acc[2 * j + 1]); }
#pragma unroll
  for (int j = 0; j < 4; ++j) { acc[2 * j] = fmaf(p3, bflo(V[3][j]), acc[2 * j]); acc[2 * j + 1] = fmaf(p3, bfhi(V[3][j]), acc[2 * j + 1]); }
  if (REFILL) {
    asm volatile("" : "+v"(acc[0]), "+v"(acc[1]), "+v"(acc[2]), "+v"(acc[3]), "+v"(acc[4]), "+v"(acc[5]), "+v"(acc[6]), "+v"(acc[7]) :: "memory");
    SCHED();
#pragma unroll
    for (int i = 0; i < 4; ++i) V[i] = ld16<AUXV>(rv, voff + i * 1024, snext);
    SCHED();
  }
}

__device__ __forceinline__ void layernorm(const u32x4 xr, u16* xnb, int lane) {
  float xv[8];
#pragma unroll
  for (int i = 0; i < 4; ++i) { xv[2 * i] = bflo(xr[i]); xv[2 * i + 1] = bfhi(xr[i]); }
  float s = ((xv[0] + xv[1]) + (xv[2] + xv[3])) + ((xv[4] + xv[5]) + (xv[6] + xv[7]));
  s = lane < 56 ? s : 0.f;
  const float mean = wavesum(s) * (1.f / D);
  float ss = 0.f;
#pragma unroll
  for (int i = 0; i < 8; ++i) { float dd = xv[i] - mean; ss = fmaf(dd, dd, ss); }
  ss = lane < 56 ? ss : 0.f;
  const float var = wavesum(ss) * (1.f / D);
  const float mb = rbf(mean);
  const float rden = __builtin_amdgcn_rcpf(rbf(__builtin_amdgcn_sqrtf(rbf(rbf(var) + 1e-5f))));
  u32x4 xp;
#pragma unroll
  for (int i = 0; i < 4; ++i) xp[i] = pkbf(rbf(xv[2 * i] - mb) * rden, rbf(xv[2 * i + 1] - mb) * rden);
  if (lane < 56) *(u32x4*)&xnb[lane * 8] = xp;
  asm volatile("s_waitcnt lgkmcnt(0)" ::: "memory");
}
template <int AUX, int I0, int N>
__device__ __forceinline__ void load_wo_part(u32x4 (&W)[32], rsrc_t rwo, u32 voff) {
#pragma unroll
  for (int i = I0; i < I0 + N; ++i) W[i] = ld16<AUX>(rwo, voff, i * (D * 2));
  SCHED();
}
template <int I0, int N>
__device__ __forceinline__ void pair_wo_part(u32x4 (&W)[32]) {
#pragma unroll
  for (int i = I0 / 2; i < (I0 + N) / 2; ++i) {
#pragma unroll
    for (int j = 0; j < 4; ++j) { u32 a = W[2 * i][j], b = W[2 * i + 1][j]; W[2 * i][j] = prlo(b, a); W[2 * i + 1][j] = prhi(b, a); }
    asm volatile("" : "+v"(W[2 * i]), "+v"(W[2 * i + 1]));
  }
  SCHED();
}

__device__ __forceinline__ void pair_wo(u32x4 (&W)[32]) {
#pragma unroll
  for (int i = 0; i < 16; ++i) {
#pragma unroll
    for (int j = 0; j < 4; ++j) { u32 a = W[2 * i][j], b = W[2 * i + 1][j]; W[2 * i][j] = prlo(b, a); W[2 * i + 1][j] = prhi(b, a); }
    asm volatile("" : "+v"(W[2 * i]), "+v"(W[2 * i + 1]));
  }
}

__global__ void __launch_bounds__(512, 2)
fused_decode_kernel(const u16* x, const u16* kc, const u16* vc, const u16* wq,
                    const u16* wo, const u16* wg, const u16* wu, const u16* wd,
                    u16* out, char* ws) {
  __shared__ __attribute__((aligned(16))) Lds sm;
  const int tid = threadIdx.x, lane = tid & 63;
  const int wave = __builtin_amdgcn_readfirstlane(tid >> 6);
  const int blk = blockIdx.x;
  const int xcd = blk & 7, idx = blk >> 3;
  const int h = 2 * xcd + (idx >> 4), b = (idx >> 2) & 3, sp = idx & 3;
  const int sq = sp;
  const int p = h * 4 + sp;
  const int lp = ((idx >> 4) ^ 1) * 4 + (idx & 3);
  const bool has_ffn = lp < 7;
  const int jk = lp < 4 ? 2 * xcd + (lp >> 1) : (lp < 6 ? 16 : 17 + (xcd >> 1));
  const int jm = lp < 6 ? (lp & 1) : (xcd & 1);
  const int j = (lp == 4 || lp == 5) && xcd < 7 ? 8 * xcd + 6 + jm : 8 * ((jk * 11) >> 5) + 2 * (jk - 3 * ((jk * 11) >> 5)) + jm;
  const rsrc_t rws = mkrsrc(ws, WS_BYTES);
  const rsrc_t rwo = mkrsrc(wo, NH * DH * D * 2);
  const int l56 = lane < 56 ? lane : 55;
  const u32 wo_voff = (u32)(h * DH + wave * 32) * (D * 2) + l56 * 16;
  u32x4 W[32];
  u32 c_gv, c_mv, c_pv, xres;
#define TAIL_SETUP() do { \
    c_gv = WS_PART + (u32)((b * 64 + 8 * wave + (lane >> 3)) * D + 8 * j + (lane & 7)) * 16; \
    c_mv = WS_ML + (u32)(b * 64 + 8 * wave + (lane >> 3)) * 16; \
    c_pv = WS_PART + (u32)((b * 64 + p) * D + tid) * 16; \
    xres = sm.xraw[(8 * j + (lane & 7)) & 511]; \
    asm volatile("" : "+v"(c_gv), "+v"(c_mv), "+v"(c_pv), "+v"(xres)); } while (0)

  if (wave < 4) {
    const int w = wave;
    if (w == 0 && lane == 0) sm.cnt = 0;
    const rsrc_t rx = mkrsrc(x, 4 * D * 2);
    const u32x4 xr = ld16<0>(rx, b * (D * 2) + l56 * 16, 0);
    const u32 ep = __hip_atomic_load((const __attribute__((address_space(1))) u32*)(ws + WS_E + b * 256), __ATOMIC_RELAXED, __HIP_MEMORY_SCOPE_AGENT);
    SCHED();
    const rsrc_t rq = mkrsrc(wq, D * NH * DH * 2);
    const u32 qcol = (h * DH + 64 * sq) * 2;
    const u32 qv = (u32)(112 * w + 2 * (lane >> 3)) * (NH * DH * 2) + qcol + (lane & 7) * 16;
    u32x4 Q[14];
#pragma unroll
    for (int i = 0; i < 14; ++i) {
      Q[i] = ld16<AUXWQ>(rq, qv, (16 * (i >> 1) + (i & 1)) * (NH * DH * 2));
      if (i & 1) SCHED();
    }
    blk_barrier();
    const u32 tag0 = ep + 1;
    if (w == 0) {
      if (lane == 0) sm.tag = tag0;
      if (lane < 56) *(u32x4*)&sm.xraw[lane * 8] = xr;
    }
    layernorm(xr, sm.xnb[w], lane);
    {
      u32 xq[7];
#pragma unroll
      for (int i = 0; i < 7; ++i) xq[i] = *(const u32*)&sm.xnb[w][112 * w + 16 * i + 2 * (lane >> 3)];
      float qa[8] = {0, 0, 0, 0, 0, 0, 0, 0};
#pragma unroll
      for (int i = 0; i < 7; ++i) {
#pragma unroll
        for (int jj = 0; jj < 4; ++jj) {
          qa[2 * jj] = dot2(prlo(Q[2 * i + 1][jj], Q[2 * i][jj]), xq[i], qa[2 * jj]);
          qa[2 * jj + 1] = dot2(prhi(Q[2 * i + 1][jj], Q[2 * i][jj]), xq[i], qa[2 * jj + 1]);
        }
      }
      float v4[4], v2[2];
#pragma unroll
      for (int e = 0; e < 4; ++e) v4[e] = sum32(qa[e], qa[e + 4]);
#pragma unroll
      for (int e = 0; e < 2; ++e) { float t = sum16(v4[e], v4[e + 2]); t += dpp<DPP_ROR8>(t); v2[e] = t; }
      if ((lane & 8) == 0) { f32x2 t = {v2[0], v2[1]}; *(f32x2*)&sm.qpart[w][8 * (lane & 7) + 4 * (lane >> 5) + 2 * ((lane >> 4) & 1)] = t; }
      asm volatile("s_waitcnt lgkmcnt(0)" ::: "memory");
      u32 old = 0;
      if (lane == 0) old = __hip_atomic_fetch_add(&sm.cnt, 1u, __ATOMIC_RELAXED, __HIP_MEMORY_SCOPE_WORKGROUP);
      old = __builtin_amdgcn_readfirstlane(old);
      MEMFENCE();
      if (old == 3) {
        const int k = lane & 15;
        const f32x4 t = (*(const f32x4*)&sm.qpart[0][4 * k] + *(const f32x4*)&sm.qpart[1][4 * k]) + (*(const f32x4*)&sm.qpart[2][4 * k] + *(const f32x4*)&sm.qpart[3][4 * k]);
        const u32x4 g = {pkbf(t[0], t[1]), tag0, pkbf(t[2], t[3]), tag0};
        const u32 qbase = WS_Q + (u32)(b * NH + h) * 1024;
        if (lane < 16) __builtin_amdgcn_raw_buffer_store_b128(g, rws, qbase + sq * 256 + k * 16, 0, 16);
        u32x4 qg;
        bool okq = false;
        for (int spin = 0; spin < (1 << 17); ++spin) {
          MEMFENCE();
          qg = ld16<16>(rws, qbase + lane * 16, 0);
          okq = __all((qg[1] == tag0) & (qg[3] == tag0));
          if (okq) break;
          __builtin_amdgcn_s_sleep(0);
        }
        u32x2 qq = {qg[0], qg[2]};
        if (!okq) { qq[0] = 0x7fc07fc0u; qq[1] = 0x7fc07fc0u; }
        *(u32x2*)&sm.qb[2 * lane] = qq;
      }
    }
    blk_barrier();
    if (has_ffn) {
      const rsrc_t rg = mkrsrc(w < 2 ? wg : wu, D * F * 2);
      const int kh = w & 1;
      const u32 gv = (u32)(224 * kh + (lane >> 2)) * (F * 2) + j * 64 + (lane & 3) * 16;
      u32x4 G[14];
#pragma unroll
      for (int i = 0; i < 14; ++i) {
        if ((j & 6) != 6) { if (i >= HIFG) vmwait<HIFG - 1>(); } else { if (i >= HIFG3) vmwait<HIFG3 - 1>(); }
        G[i] = ld16<AUXWF>(rg, gv, i * 16 * (F * 2));
      }
      const rsrc_t rd = mkrsrc(wd, F * D * 2);
      u32x4 Dn[8];
#pragma unroll
      for (int i = 0; i < 8; ++i) { INFLIGHT(14 + i, HIFG); Dn[i] = ld16<AUXWF>(rd, (u32)(32 * j + 8 * w) * (D * 2) + l56 * 16, i * (D * 2)); }
      MEMFENCE();
      float ga[8] = {0, 0, 0, 0, 0, 0, 0, 0};
#pragma unroll
      for (int i = 0; i < 7; ++i) {
        const int k0 = 224 * kh + 32 * i + (lane >> 2);
        const u32 xx = (u32)sm.xnb[w][k0] | ((u32)sm.xnb[w][k0 + 16] << 16);
#pragma unroll
        for (int jj = 0; jj < 4; ++jj) {
          ga[2 * jj] = dot2(prlo(G[2 * i + 1][jj], G[2 * i][jj]), xx, ga[2 * jj]);
          ga[2 * jj + 1] = dot2(prhi(G[2 * i + 1][jj], G[2 * i][jj]), xx, ga[2 * jj + 1]);
        }
      }
      float v4[4], v2[2];
#pragma unroll
      for (int e = 0; e < 4; ++e) v4[e] = sum32(ga[e], ga[e + 4]);
#pragma unroll
      for (int e = 0; e < 2; ++e) { float t = sum16(v4[e], v4[e + 2]); t += dpp<DPP_ROR8>(t); t += dpp<DPP_ROR4>(t); v2[e] = t; }
      if ((lane & 12) == 0) { f32x2 t = {v2[0], v2[1]}; *(f32x2*)&sm.gup[w][8 * (lane & 3) + 2 * ((lane >> 4) & 1) + 4 * (lane >> 5)] = t; }
      asm volatile("s_waitcnt lgkmcnt(0)" ::: "memory");
      if (lane == 0) __hip_atomic_fetch_add(&sm.cnt, 1u, __ATOMIC_RELAXED, __HIP_MEMORY_SCOPE_WORKGROUP);
      while (__hip_atomic_load(&sm.cnt, __ATOMIC_RELAXED, __HIP_MEMORY_SCOPE_WORKGROUP) < 8u) __builtin_amdgcn_s_sleep(1);
      MEMFENCE();
      const int cc = lane & 31;
      const float g = rbf(sm.gup[0][cc] + sm.gup[1][cc]);
      const float u = rbf(sm.gup[2][cc] + sm.gup[3][cc]);
      const float inner = 0.7978845608028654f * (g + 0.044715f * g * g * g);
      const float th = 1.f - 2.f * __builtin_amdgcn_rcpf(__builtin_amdgcn_exp2f(inner * 2.8853900817779268f) + 1.f);
      const float ge = rbf(0.5f * g * (1.f + th));
      const float hv = ge * u;
      const u32 hp = pkbf(hv, dpp<DPP_X1>(hv));
      float za[8] = {0, 0, 0, 0, 0, 0, 0, 0};
#pragma unroll
      for (int i = 0; i < 4; ++i) {
        const u32 hh = __builtin_amdgcn_readlane(hp, 8 * w + 2 * i);
#pragma unroll
        for (int jj = 0; jj < 4; ++jj) {
          za[2 * jj] = dot2(prlo(Dn[2 * i + 1][jj], Dn[2 * i][jj]), hh, za[2 * jj]);
          za[2 * jj + 1] = dot2(prhi(Dn[2 * i + 1][jj], Dn[2 * i][jj]), hh, za[2 * jj + 1]);
        }
      }
      if (lane < 56) {
        f32x4 a = {za[0], za[1], za[2], za[3]}, bb = {za[4], za[5], za[6], za[7]};
        *(f32x4*)&sm.zpart[w][8 * lane] = a; *(f32x4*)&sm.zpart[w][8 * lane + 4] = bb;
      }
    }
#pragma unroll
    for (int i = 0; i < 32; ++i) {
      INFLIGHT(i, HIFW);
      W[i] = ld16<AUXWO>(rwo, wo_voff, i * (D * 2));
    }
    SCHED();
    pair_wo(W);
    if (w == 0 && has_ffn && j == 0) {
      const u32 qv0 = WS_Q + (u32)(b * NH + (lane >> 2)) * 1024 + (lane & 3) * 256;
      for (int spin = 0; spin < (1 << 17); ++spin) {
        MEMFENCE();
        const u32x4 t = ld16<16>(rws, qv0, 0);
        if (__all((t[1] == tag0) & (t[3] == tag0))) break;
        __builtin_amdgcn_s_sleep(4);
      }
      if (lane == 0) __hip_atomic_store((__attribute__((address_space(1))) u32*)(ws + WS_E + b * 256), tag0, __ATOMIC_RELAXED, __HIP_MEMORY_SCOPE_AGENT);
    }
    TAIL_SETUP();
  } else {
    const int sw = wave - 4, c = lane & 31, r = lane >> 5;
    __builtin_amdgcn_s_setprio(SPRIO);
    const size_t kvoff = ((size_t)(b * NH + h) * SK + sp * 1024) * DH;
    const rsrc_t rk = mkrsrc(kc + kvoff, 1024 * DH * 2);
    const rsrc_t rv = mkrsrc(vc + kvoff, 1024 * DH * 2);
    const u32 voff = sw * 4096 + lane * 16;
    u32x4 KT[PFD][4], VT[PFD][4];
#define HALF(u) do { if ((u) < 2 * PFD) { if ((u) & 1) { _Pragma("unroll") for (int i = 0; i < 4; ++i) VT[(u) >> 1][i] = ld16<AUXV>(rv, voff + i * 1024, ((u) >> 1) * 16384); } \
                                           else { _Pragma("unroll") for (int i = 0; i < 4; ++i) KT[(u) >> 1][i] = ld16<AUXK>(rk, voff + i * 1024, ((u) >> 1) * 16384); } SCHED(); } } while (0)
#define PACE(u) do { if ((u) < 2 * PFD) { asm volatile("s_waitcnt vmcnt(%0)" :: "n"(4 * (PFI - 1)) : "memory"); HALF(u); } } while (0)
    HALF(0); HALF(1);
    HALF(2);
    blk_barrier();
    PACE(3);
    PACE(4); PACE(5); PACE(6); PACE(7); PACE(8); PACE(9); PACE(10); PACE(11);
    blk_barrier();
    u32 q[4];
    { const u32x4 t = *(const u32x4*)&sm.qb[4 * c]; q[0] = t[0]; q[1] = t[1]; q[2] = t[2]; q[3] = t[3]; }
    TAIL_SETUP();
    const bool b4 = (lane & 4) != 0, b8 = (lane & 8) != 0;
    float m = -INFINITY, l = 0.f, acc[8] = {0, 0, 0, 0, 0, 0, 0, 0};
#define ONE(t) tile<true>(KT[0], VT[0], q, m, l, acc, rk, rv, voff, ((t) + 1) * 16384, b4, b8)
#define RUN(t0, n) do { for (int t = (t0); t < (t0) + (n); ++t) ONE(t); } while (0)
#define RUNP(t0, n) do { ONE(t0); SCHED(); RUN((t0) + 1, (n) - 1); } while (0)
    tile<true>(KT[0], VT[0], q, m, l, acc, rk, rv, voff, PFD * 16384, b4, b8);
    tile<false>(KT[1], VT[1], q, m, l, acc, rk, rv, voff, 0, b4, b8); SCHED();
    tile<false>(KT[2], VT[2], q, m, l, acc, rk, rv, voff, 0, b4, b8); SCHED();
    tile<false>(KT[3], VT[3], q, m, l, acc, rk, rv, voff, 0, b4, b8); SCHED();
    tile<false>(KT[4], VT[4], q, m, l, acc, rk, rv, voff, 0, b4, b8); SCHED();
    tile<false>(KT[5], VT[5], q, m, l, acc, rk, rv, voff, 0, b4, b8); SCHED();
    RUN(PFD, WST);
    load_wo_part<AUXWO, 0, 8>(W, rwo, wo_voff);
    RUNP(PFD + WST, WOCH);
    pair_wo_part<0, 8>(W);
    load_wo_part<AUXWO, 8, 8>(W, rwo, wo_voff);
    RUNP(PFD + WST + WOCH, WOCH);
    pair_wo_part<8, 8>(W);
    load_wo_part<AUXWO, 16, 8>(W, rwo, wo_voff);
    RUNP(PFD + WST + 2 * WOCH, WOCH);
    pair_wo_part<16, 8>(W);
    load_wo_part<AUXWO, 24, 8>(W, rwo, wo_voff);
    RUNP(PFD + WST + 3 * WOCH, 2);
    pair_wo_part<24, 8>(W);
    RUN(PFD + WST + 3 * WOCH + 2, 31 - (PFD + WST + 3 * WOCH + 2));
    tile<false>(KT[0], VT[0], q, m, l, acc, rk, rv, voff, 0, b4, b8);
    const float M = max32(m);
    const float sc = __builtin_amdgcn_exp2f((m - M) * CEXP);
    l *= sc;
    l += dpp<DPP_ROR4>(l); l += dpp<DPP_ROR8>(l);
    l = sum32(l, l);
    f32x4 o4;
#pragma unroll
    for (int e = 0; e < 4; ++e) o4[e] = sum32(acc[e] * sc, acc[e + 4] * sc);
    *(f32x4*)&sm.accp[sw][8 * c + 4 * r] = o4;
    if (lane == 0) { f32x2 t = {M, l}; *(f32x2*)&sm.ml[sw][0] = t; }
  }
  blk_barrier();
  const u32 tag = sm.tag;
  float zp[4];
  {
    const int r = lane >> 4;
    const f32x2 mlr = *(const f32x2*)&sm.ml[r][0];
    const f32x2 ap = *(const f32x2*)&sm.accp[r][32 * wave + 2 * (lane & 15)];
    const float Mb = max32(max16(mlr[0]));
    const float er = __builtin_amdgcn_exp2f((mlr[0] - Mb) * CEXP);
    float o0 = er * ap[0], o1 = er * ap[1];
    o0 = sum16(o0, o0); o1 = sum16(o1, o1);
    o0 = sum32(o0, o0); o1 = sum32(o1, o1);
    u32 op = pkbf(o0, o1);
    if (wave == 0) {
      float Lb = er * mlr[1];
      Lb = sum16(Lb, Lb); Lb = sum32(Lb, Lb);
      const u32x4 g = {asu(Mb), tag, asu(Lb), tag};
      if (lane == 0) __builtin_amdgcn_raw_buffer_store_b128(g, rws, WS_ML + (b * 64 + p) * 16, 0, 16);
    }
    float y[8] = {0, 0, 0, 0, 0, 0, 0, 0};
    const int tz = tid < D ? tid : 0;
#pragma unroll
    for (int i = 0; i < 4; ++i) zp[i] = sm.zpart[i][tz];
    asm volatile("s_nop 1" : "+v"(op));
#define PROJ(i) _Pragma("unroll") for (int jj = 0; jj < 4; ++jj) { \
      asm("v_dot2c_f32_bf16_dpp %0, %1, %2 row_newbcast:" #i " row_mask:0xf bank_mask:0xf" : "+v"(y[2 * jj]) : "v"(op), "v"(W[2 * i][jj])); \
      asm("v_dot2c_f32_bf16_dpp %0, %1, %2 row_newbcast:" #i " row_mask:0xf bank_mask:0xf" : "+v"(y[2 * jj + 1]) : "v"(op), "v"(W[2 * i + 1][jj])); }
    PROJ(0) PROJ(1) PROJ(2) PROJ(3) PROJ(4) PROJ(5) PROJ(6) PROJ(7)
    PROJ(8) PROJ(9) PROJ(10) PROJ(11) PROJ(12) PROJ(13) PROJ(14) PROJ(15)
    if (lane < 56) {
      f32x4 a = {y[0], y[1], y[2], y[3]}, bb = {y[4], y[5], y[6], y[7]};
      *(f32x4*)&sm.ypart[wave][8 * lane] = a; *(f32x4*)&sm.ypart[wave][8 * lane + 4] = bb;
    }
  }
  float zsum = has_ffn ? (zp[0] + zp[1]) + (zp[2] + zp[3]) : 0.f;
  asm volatile("" : "+v"(zsum));
  blk_barrier();
  if (tid < D) {
    float y = ((sm.ypart[0][tid] + sm.ypart[1][tid]) + (sm.ypart[2][tid] + sm.ypart[3][tid])) + ((sm.ypart[4][tid] + sm.ypart[5][tid]) + (sm.ypart[6][tid] + sm.ypart[7][tid]));
    u32x4 g = {asu(y), tag, asu(zsum), tag};
    __builtin_amdgcn_raw_buffer_store_b128(g, rws, c_pv, 0, 16);
  }
  if (has_ffn) {
    const u32 gv = c_gv, mv = c_mv;
    u32x4 g = {0, ~tag, 0, ~tag}, mg;
    bool vd = false, okm, ok;
    int spin = 0;
#pragma nounroll
    do {
      MEMFENCE();
      mg = ld16<16>(rws, mv, 0);
      if (!vd) g = ld16<16>(rws, gv, 0);
      MEMFENCE();
      vd = (g[1] == tag) & (g[3] == tag);
      okm = __all((mg[1] == tag) & (mg[3] == tag));
    } while (!okm && ++spin < (1 << 17));
    const float mp = asf(mg[0]), Lp = asf(mg[2]);
    const float Mh = max16(fmaxf(mp, dpp<DPP_ROR8>(mp)));
    const float ep = __builtin_amdgcn_exp2f((mp - Mh) * CEXP);
    float Lh = ep * Lp; Lh += dpp<DPP_ROR8>(Lh); Lh = sum16(Lh, Lh);
    float wp = ep * __builtin_amdgcn_rcpf(Lh);
    asm volatile("" : "+v"(wp));
    ok = __all(vd);
#pragma nounroll
    while (!ok && ++spin < (1 << 17)) {
      MEMFENCE();
      if (!vd) g = ld16<16>(rws, gv, 0);
      MEMFENCE();
      vd = (g[1] == tag) & (g[3] == tag);
      ok = __all(vd);
    }
    float Y = wp * asf(g[0]), Z = asf(g[2]);
    Y += dpp<DPP_ROR8>(Y); Z += dpp<DPP_ROR8>(Z);
    Y = sum16(Y, Z);
    Y = sum32(Y, Y);
    if (!(ok & okm)) Y = __builtin_nanf("");
    if ((lane & 0x28) == 0) sm.fin[wave][(lane >> 4) * 8 + (lane & 7)] = Y;
    blk_barrier();
    if (wave == 0) {
      float Yf = sm.fin[lane >> 3][lane & 7], Zf = sm.fin[lane >> 3][8 + (lane & 7)];
      Yf += dpp<DPP_ROR8>(Yf); Zf += dpp<DPP_ROR8>(Zf);
      Yf = sum16(Yf, Zf);
      Yf = sum32(Yf, Yf);
      const float Zl = asf(__builtin_amdgcn_permlane16_swap(asu(Yf), asu(Yf), false, false)[1]);
      const float res = rbf(rbf(bflo(xres) + rbf(Yf)) + rbf(Zl));
      if (lane < 8) out[b * D + 8 * j + lane] = (u16)(pkbf(res, 0.f) & 0xffffu);
    }
  }
}

extern "C" void kernel_launch(void* const* d_in, const int* in_sizes, int n_in, void* d_out, int out_size, void* d_ws, size_t ws_size,
                              hipStream_t stream) {
  fused_decode_kernel<<<256, 512, 0, stream>>>((const u16*)d_in[0], (const u16*)d_in[1], (const u16*)d_in[2], (const u16*)d_in[3],
                                               (const u16*)d_in[4], (const u16*)d_in[6], (const u16*)d_in[7], (const u16*)d_in[8],
                                               (u16*)d_out, (char*)d_ws);
}
```
